```python
import torch

def _softmax(x, axis=-1):
    return torch.softmax(x, dim=axis)
def _gelu(x):
    return torch.nn.functional.gelu(x, approximate="tanh")
def _silu(x):
    return torch.nn.functional.silu(x)
def _sigmoid(x):
    return torch.sigmoid(x)
def _layernorm(x, axis=-1, eps=1e-5):
    m = x.mean(dim=axis, keepdim=True)
    v = x.var(dim=axis, keepdim=True, unbiased=False)
    return (x - m) / torch.sqrt(v + eps)
def _scatter_add(tgt, idx, src, axis=0):
    return tgt.clone().index_add_(axis, idx, src)
def _segment_sum(src, seg, n):
    out = torch.zeros((n,) + src.shape[1:], dtype=src.dtype)
    return out.index_add_(0, seg, src)
def _pad(x, pw):
    flat = tuple(v for pair in reversed(pw) for v in pair)
    return torch.nn.functional.pad(x, flat)

kernel_name = 'concat_heads__ssm_conv_h3__mha_block__prompt'

def setup_inputs():

    _w = (lambda a: (lambda t: t.to(torch.bfloat16) if t.is_floating_point() else t)(torch.as_tensor(a)))
    def _A_setup():
        B, S, D = 1, 2048, 1024
        ks = 4
        return {
            "q": _w((lambda *s: torch.randn(*s, dtype=torch.bfloat16) * 0.05)(B, S, D)),
            "k": _w((lambda *s: torch.randn(*s, dtype=torch.bfloat16) * 0.8)(B, S, D)),
            "v": _w((lambda *s: torch.randn(*s, dtype=torch.bfloat16) * 0.8)(B, S, D)),
            "shift_k": _w((lambda *s: torch.randn(*s, dtype=torch.bfloat16) * 0.8)(D, ks) * 0.5),
            "ssm_k": _w((lambda *s: torch.randn(*s, dtype=torch.bfloat16) * 0.8)(D, S) * (1.0 / S ** 0.5)),
            "ssm_D": _w((lambda *s: torch.randn(*s, dtype=torch.bfloat16) * 0.8)(D)),
        }
    _A_in = _A_setup()

    def _B_setup():
        B, S, D, H = 1, 2048, 1024, 32
        return {
            "x": _w((lambda *s: torch.randn(*s, dtype=torch.bfloat16) * 0.2)(B, S, D)),
            "wqkv": _w((lambda *s: torch.randn(*s, dtype=torch.bfloat16) * 0.2)(D, 3 * D)),
            "wo": _w((lambda *s: torch.randn(*s, dtype=torch.bfloat16) * 0.05)(D, D)),
        }
    _B_in = _B_setup()

    return {
        "A_q": _A_in["q"],
        "A_k": _A_in["k"],
        "A_v": _A_in["v"],
        "A_shift_k": _A_in["shift_k"],
        "A_ssm_k": _A_in["ssm_k"],
        "A_ssm_D": _A_in["ssm_D"],
        "B_x": _B_in["x"],
        "B_wqkv": _B_in["wqkv"],
        "B_wo": _B_in["wo"],
    }

def reference(A_q, A_k, A_v, A_shift_k, A_ssm_k, A_ssm_D, B_x, B_wqkv, B_wo):
    def _A_ref(q, k, v, shift_k, ssm_k, ssm_D):
        B, S, D = q.shape
        ks = shift_k.shape[-1]
        k_pad = _pad(k, ((0, 0), (ks - 1, 0), (0, 0)))
        k_shift = torch.zeros_like(k)
        for t in range(ks):
            k_shift = k_shift + k_pad[:, t:t + S, :] * shift_k[:, ks - 1 - t][None, None, :]
        kv = k_shift * v
        L = 2 * S
        kv_pad = _pad(kv, ((0, 0), (0, S), (0, 0)))
        kf_pad = _pad(ssm_k, ((0, 0), (0, S)))
        kv_f = (lambda x, axes=(-2,-1): torch.fft.rfft2(x.float(), dim=axes))(kv_pad[:, None, :, :], axes=(1, 2))
        kf_f = (lambda x, axes=(-2,-1): torch.fft.rfft2(x.float(), dim=axes))(kf_pad[:, None, :], axes=(1, 2))
        kf_f = (lambda x, *d: x.permute(*d))(kf_f, 1, 2, 0)
        y_f = kv_f * kf_f[None, :, :, :]
        y = (lambda x, s, axes=(-2,-1): torch.fft.irfft2(x, s=s, dim=axes).to(torch.bfloat16))(y_f, (1, L), axes=(1, 2))[:, 0, :S, :]
        y = y + kv * ssm_D[None, None, :]
        return q * y

    def _B_ref(x, wqkv, wo):
        B, S, D = x.shape
        H = 32
        Dh = D // H
        qkv = (lambda x, *d: x.permute(*d))((x @ wqkv).reshape(B, S, 3, H, Dh), 2, 0, 3, 1, 4)
        q, k, v = qkv[0], qkv[1], qkv[2]
        scores = torch.einsum("bhqd,bhkd->bhqk", q, k) * (Dh ** -0.5)
        p = _softmax(scores, axis=-1)
        o = torch.einsum("bhqk,bhkd->bhqd", p, v)
        return (lambda x, *d: x.permute(*d))(o, 0, 2, 1, 3).reshape(B, S, D) @ wo

    a = _A_ref(A_q, A_k, A_v, A_shift_k, A_ssm_k, A_ssm_D)
    b = _B_ref(B_x, B_wqkv, B_wo)
    return torch.concatenate([a, b], axis=-1)
```

```cpp
#include <hip/hip_runtime.h>
#include <hip/hip_bf16.h>
typedef unsigned short u16;
__device__ __forceinline__ float b2f(u16 b){ return __uint_as_float(((unsigned)b)<<16); }
__device__ __forceinline__ u16 f2b(float f){ unsigned u=__float_as_uint(f); u += 0x7fffu + ((u>>16)&1u); return (u16)(u>>16); }
constexpr int S=2048, D=1024, H=32, DH=32;

__global__ void k_kv(const u16* k, const u16* v, const u16* sk, u16* kv){
  int i = blockIdx.x*blockDim.x+threadIdx.x; int t=i/D, c=i%D;
  float acc=0.f;
  for(int j=0;j<4;++j){ int tt=t-3+j; float kk = tt>=0? b2f(k[tt*D+c]):0.f; float p=b2f(f2b(kk*b2f(sk[c*4+3-j]))); acc=b2f(f2b(acc+p)); }
  kv[i]=f2b(acc*b2f(v[i]));
}
__global__ void k_conv(const u16* q,const u16* kv,const u16* h,const u16* Dp,u16* out){
  int i = blockIdx.x*blockDim.x+threadIdx.x; int t=i/D, c=i%D;
  float acc=0.f;
  for(int s=0;s<=t;++s) acc += b2f(h[c*S+(t-s)])*b2f(kv[s*D+c]);
  float y=b2f(f2b(acc));
  float z=b2f(f2b(b2f(kv[i])*b2f(Dp[c])));
  y=b2f(f2b(y+z));
  out[t*2048+c]=f2b(b2f(q[i])*y);
}
__global__ void k_gemm(const u16* A,const u16* B,u16* C,int M,int N,int K,int ldc){
  int i = blockIdx.x*blockDim.x+threadIdx.x; int m=i/N, n=i%N;
  float acc=0.f;
  for(int k=0;k<K;++k) acc+=b2f(A[m*K+k])*b2f(B[k*N+n]);
  C[m*ldc+n]=f2b(acc);
}
__global__ void k_attn(const u16* qkv,u16* o){
  int i = blockIdx.x*blockDim.x+threadIdx.x; int t=i%S, h=i/S;
  float q[DH]; for(int d=0;d<DH;++d) q[d]=b2f(qkv[t*3072+h*DH+d]);
  float m=-1e30f,l=0.f; float acc[DH]; for(int d=0;d<DH;++d) acc[d]=0.f;
  for(int j=0;j<S;++j){ float s=0.f; for(int d=0;d<DH;++d) s+=q[d]*b2f(qkv[j*3072+1024+h*DH+d]); s*=0.17677669529f;
    float mn=fmaxf(m,s); float a=__expf(m-mn); float p=__expf(s-mn); l=l*a+p;
    for(int d=0;d<DH;++d) acc[d]=acc[d]*a+p*b2f(qkv[j*3072+2048+h*DH+d]); m=mn; }
  for(int d=0;d<DH;++d) o[t*D+h*DH+d]=f2b(acc[d]/l);
}
extern "C" void kernel_launch(void* const* d_in, const int* in_sizes, int n_in, void* d_out, int out_size, void* d_ws, size_t ws_size, hipStream_t stream) {
  const u16* q=(const u16*)d_in[0]; const u16* k=(const u16*)d_in[1]; const u16* v=(const u16*)d_in[2];
  const u16* sk=(const u16*)d_in[3]; const u16* h=(const u16*)d_in[4]; const u16* Dp=(const u16*)d_in[5];
  const u16* x=(const u16*)d_in[6]; const u16* wqkv=(const u16*)d_in[7]; const u16* wo=(const u16*)d_in[8];
  u16* out=(u16*)d_out; u16* ws=(u16*)d_ws;
  u16* kv=ws; u16* qkv=ws+S*D; u16* o=qkv+S*3072;
  k_kv<<<S*D/256,256,0,stream>>>(k,v,sk,kv);
  k_conv<<<S*D/256,256,0,stream>>>(q,kv,h,Dp,out);
  k_gemm<<<S*3072/256,256,0,stream>>>(x,wqkv,qkv,S,3072,1024,3072);
  k_attn<<<S*H/64,64,0,stream>>>(qkv,o);
  k_gemm<<<S*1024/256,256,0,stream>>>(o,wo,out+1024,S,1024,1024,2048);
}
```

```cpp
#include <hip/hip_runtime.h>
#include <hip/hip_bf16.h>
#include <stdint.h>

typedef unsigned short u16;
using bf16x8 = __attribute__((ext_vector_type(8))) short;
using s16x4  = __attribute__((ext_vector_type(4))) short;
using f32x16 = __attribute__((ext_vector_type(16))) float;
using f32x4  = __attribute__((ext_vector_type(4))) float;
using u32x4  = __attribute__((ext_vector_type(4))) unsigned;
using u32x2  = __attribute__((ext_vector_type(2))) unsigned;
typedef short v4i16_t __attribute__((ext_vector_type(4)));
#define LAS __attribute__((address_space(3)))
#define GAS __attribute__((address_space(1)))
#define DI __device__ __forceinline__
typedef LAS const char* lds_cptr;

constexpr int S = 2048, D = 1024, H = 32;
constexpr float QSCALE = 0.17677669529663687f * 1.4426950408889634f;

#define MFMA32(a, b, c) __builtin_amdgcn_mfma_f32_32x32x16_bf16((a), (b), (c), 0, 0, 0)
#define MFMA16(a, b, c) __builtin_amdgcn_mfma_f32_16x16x32_bf16((a), (b), (c), 0, 0, 0)

typedef float f32x2_t __attribute__((ext_vector_type(2)));
typedef __bf16 bf16x2_t __attribute__((ext_vector_type(2)));
DI unsigned cvtpk(float lo, float hi) { f32x2_t v = {lo, hi}; bf16x2_t b = __builtin_convertvector(v, bf16x2_t); return __builtin_bit_cast(unsigned, b); }
DI float bflo(unsigned p) { return __uint_as_float(p << 16); }
DI float bfhi(unsigned p) { return __uint_as_float(p & 0xffff0000u); }
DI float rbf(float x) { return bflo(cvtpk(x, x)); }
DI bf16x8 ldsb128(lds_cptr p) { return *(const LAS bf16x8*)p; }
DI s16x4 ldstr(lds_cptr p) { return __builtin_bit_cast(s16x4, __builtin_amdgcn_ds_read_tr16_b64_v4i16((LAS v4i16_t*)p)); }
DI bf16x8 cat8(s16x4 lo, s16x4 hi) { return __builtin_shufflevector(lo, hi, 0, 1, 2, 3, 4, 5, 6, 7); }
DI void glds16(const void* g, LAS char* l) { __builtin_amdgcn_global_load_lds((const GAS unsigned*)g, (LAS unsigned*)l, 16, 0, 0); }
DI bf16x8 pack8(const f32x16& x, int b) {
  u32x4 p; p[0] = cvtpk(x[b], x[b + 1]); p[1] = cvtpk(x[b + 2], x[b + 3]); p[2] = cvtpk(x[b + 4], x[b + 5]); p[3] = cvtpk(x[b + 6], x[b + 7]);
  return __builtin_bit_cast(bf16x8, p);
}
DI void bdma(__amdgpu_buffer_rsrc_t srd, unsigned voff, unsigned soff, unsigned m0v) {
  asm volatile("s_mov_b32 m0, %0\n\ts_nop 0\n\tbuffer_load_dwordx4 %1, %2, %3 offen lds" :: "s"(m0v), "v"(voff), "s"(srd), "s"(soff) : "m0", "memory");
}
#define RSRC(p, bytes) __builtin_amdgcn_make_buffer_rsrc((void*)(p), (short)0, (int)(bytes), 0x00020000)
#define WAITV(n) asm volatile("s_waitcnt vmcnt(" #n ")" ::: "memory")
#define BAR() asm volatile("s_waitcnt lgkmcnt(0)\n\ts_barrier" ::: "memory")

#define STAMP_DECL
#define STAMP_AT(i)
#define STAMP_RT(i)
#define STAMP_OUT(buf, kern)
#define SB() __builtin_amdgcn_sched_barrier(0)
#define BARS() do { SB(); BAR(); SB(); } while (0)
#define ST_MODE 2
DI void st16(void* p, bf16x8 v) {
  asm volatile("global_store_dwordx4 %0, %1, off sc1\n\ts_nop 1" :: "v"(p), "v"(v) : "memory");
}
DI void st8(void* p, u32x2 v) {
  asm volatile("global_store_dwordx2 %0, %1, off sc1" :: "v"(p), "v"(v) : "memory");
}
#define INIT_ST 2
#define LD_UNROLL 1
#define RDPAT 1
#define PRIO 0
#define USE_BUF 1
#define PRE_AT 0
#define PF_MODE 1
constexpr int G1_SLOT = 40960, G1_NSLOT = 4, G1_LDS = G1_SLOT * G1_NSLOT;
__global__ __launch_bounds__(640, 3) void k1_qkv(const u16* __restrict__ x, const u16* __restrict__ w, const u16* __restrict__ kin, const u16* __restrict__ vin,
                                                 const u16* __restrict__ sk, u16* __restrict__ Qf, u16* __restrict__ KVf, float* __restrict__ kn2, u16* __restrict__ kvT) {
  extern __shared__ __attribute__((aligned(16))) char smem[];
  LAS char* lds = (LAS char*)smem;
  const int tid = threadIdx.x, lane = tid & 63, wid = __builtin_amdgcn_readfirstlane(tid >> 6);
  const int b = blockIdx.x, xcd = b & 7, idx = b >> 3;
  const int tb = (xcd & 1) * 8 + (idx & 7), hp = (xcd >> 1) * 4 + (idx >> 3);
  if (wid >= 8) {
    const int gw = b * 2 + (wid - 8), tp = gw >> 4, cp = gw & 15;
    const int t0 = tp * 64 + 8 * (lane >> 3), c0 = cp * 64 + 8 * (lane & 7);
    u32x4 kr[11], vr[8], sw[4];
    const u16* kp = kin + (size_t)t0 * D + c0; const u16* vp = vin + (size_t)t0 * D + c0;
#define PB0
    PB0
#pragma unroll
    for (int i = 0; i < 4; ++i) sw[i] = *(const u32x4*)(sk + (size_t)c0 * 4 + 8 * i);
#pragma unroll
    for (int i = 0; i < 11; ++i) kr[i] = *(const u32x4*)(t0 - 3 + i < 0 ? kp : kp + (i - 3) * D);
#pragma unroll
    for (int i = 0; i < 8; ++i) vr[i] = *(const u32x4*)(vp + i * D);
    BARS();
    BARS(); BARS(); BARS();
#pragma unroll
    for (int i = 0; i < 3; ++i) if (t0 - 3 + i < 0) kr[i] = (u32x4){0, 0, 0, 0};
#pragma unroll
    for (int cp2 = 0; cp2 < 4; ++cp2) {
      f32x2_t wt[4];
      wt[0] = (f32x2_t){bflo(sw[cp2][0]), bflo(sw[cp2][2])}; wt[1] = (f32x2_t){bfhi(sw[cp2][0]), bfhi(sw[cp2][2])};
      wt[2] = (f32x2_t){bflo(sw[cp2][1]), bflo(sw[cp2][3])}; wt[3] = (f32x2_t){bfhi(sw[cp2][1]), bfhi(sw[cp2][3])};
      f32x2_t o[8];
#pragma unroll
      for (int h2 = 0; h2 < 2; ++h2) {
        f32x2_t kk[7];
#pragma unroll
        for (int i = 0; i < 7; ++i) kk[i] = (f32x2_t){bflo(kr[4 * h2 + i][cp2]), bfhi(kr[4 * h2 + i][cp2])};
#pragma unroll
        for (int i = 0; i < 4; ++i) {
          f32x2_t a = kk[i] * wt[3]; a = kk[i + 1] * wt[2] + a; a = kk[i + 2] * wt[1] + a; a = kk[i + 3] * wt[0] + a;
          o[4 * h2 + i] = a * (f32x2_t){bflo(vr[4 * h2 + i][cp2]), bfhi(vr[4 * h2 + i][cp2])};
        }
        BARS();
      }
#pragma unroll
      for (int e = 0; e < 2; ++e) {
        const u32x4 pk = {cvtpk(o[0][e], o[1][e]), cvtpk(o[2][e], o[3][e]), cvtpk(o[4][e], o[5][e]), cvtpk(o[6][e], o[7][e])};
        st16(kvT + (size_t)(c0 + 2 * cp2 + e) * S + t0, __builtin_bit_cast(bf16x8, pk));
      }
    }
    BARS(); BARS(); BARS(); BARS();
    return;
  }
  if (wid >= 4) {
    const int lw = wid - 4;
    unsigned xoff[4], woff[6];
#pragma unroll
    for (int i = 0; i < 4; ++i) { const int row = 32 * lw + 8 * i + (lane >> 3), ch = (lane & 7) ^ ((row >> 1) & 7); xoff[i] = (unsigned)((tb * 128 + row) * 2048 + ch * 16); }
#pragma unroll
    for (int i = 0; i < 6; ++i) { const int p = 6 * lw + i, sec = p >> 3, row = 8 * (p & 7) + (lane >> 3), ch = (lane & 7) ^ (((row >> 1) & 1) << 2);
      woff[i] = (unsigned)(row * 6144 + sec * 2048 + hp * 128 + ch * 16); }
    const __amdgpu_buffer_rsrc_t rx = RSRC(x, S * D * 2), rw = RSRC(w, D * 3 * D * 2);
    auto issue = [&](int st) {
      const unsigned sl = (unsigned)(st & 3) * G1_SLOT + 4096u * lw, sw_ = (unsigned)(st & 3) * G1_SLOT + 16384u + 6144u * lw;
#pragma unroll
      for (int i = 0; i < 4; ++i) bdma(rx, xoff[i], (unsigned)st * 128u, sl + i * 1024);
#pragma unroll
      for (int i = 0; i < 6; ++i) bdma(rw, woff[i], (unsigned)st * (64u * 6144u), sw_ + i * 1024);
    };
    issue(0); issue(1); WAITV(10); BAR(); issue(2); issue(3);
    WAITV(20); BAR();
#pragma unroll LD_UNROLL
    for (int s = 1; s < 13; ++s) { issue(s + 3); WAITV(20); BAR(); }
    WAITV(10); BAR();
    WAITV(0); BAR();
    return;
  }
  STAMP_DECL STAMP_RT(6); STAMP_AT(0);
  const int wm = wid & 1, wn = wid >> 1, r32 = lane & 31, hi = lane >> 5;
  f32x16 acc[2][3];
#pragma unroll
  for (int i = 0; i < 2; ++i)
#pragma unroll
    for (int j = 0; j < 3; ++j) acc[i][j] = f32x16{};
  const int xrow = wm * 64 + r32, xsw = (xrow >> 1) & 7;
  const int xb0 = xrow * 128 + ((hi ^ xsw) << 4);
  const int q4 = (lane & 15) >> 2, p4 = lane & 3, g1 = (lane >> 4) & 1;
  const int wb0 = 16384 + (8 * hi + q4) * 128 + ((wn ^ (q4 >> 1)) << 6) + 32 * g1 + 8 * p4;
  bf16x8 xa[2][2]; s16x4 wl[2][3], wh[2][3];
#define RD(...) __VA_ARGS__
  auto step = [&](int cur, bool mm, bool rd, int so, int ks) {
    const int nx = cur ^ 1;
    lds_cptr xp = lds + so + (xb0 ^ (ks << 5));
    lds_cptr wp = lds + so + wb0 + ks * 2048;
    bf16x8 w0, w1, w2;
    if (mm) { w0 = cat8(wl[cur][0], wh[cur][0]); w1 = cat8(wl[cur][1], wh[cur][1]); w2 = cat8(wl[cur][2], wh[cur][2]); }
#define RX0 xa[nx][0] = ldsb128(xp);
#define RX1 xa[nx][1] = ldsb128(xp + 4096);
#define RWL(i) wl[nx][i] = ldstr(wp + (i) * 8192);
#define RWH(i) wh[nx][i] = ldstr(wp + (i) * 8192 + 512);
#define G0 RX0 RWL(0)
#define G1 RWH(0) RWL(1)
#define G2 RWH(1) RWL(2)
#define G3 RWH(2) RX1
#define G4
#define G5
    if (mm) acc[0][0] = MFMA32(w0, xa[cur][0], acc[0][0]);
    SB(); if (rd) { RD(G0) } SB();
    if (mm) acc[0][1] = MFMA32(w1, xa[cur][0], acc[0][1]);
    SB(); if (rd) { RD(G1) } SB();
    if (mm) acc[0][2] = MFMA32(xa[cur][0], w2, acc[0][2]);
    SB(); if (rd) { RD(G2) } SB();
    if (mm) acc[1][0] = MFMA32(w0, xa[cur][1], acc[1][0]);
    SB(); if (rd) { RD(G3) } SB();
    if (mm) acc[1][1] = MFMA32(w1, xa[cur][1], acc[1][1]);
    SB(); if (rd) { RD(G4) } SB();
    if (mm) acc[1][2] = MFMA32(xa[cur][1], w2, acc[1][2]);
    SB(); if (rd) { RD(G5) } SB();
  };
  BAR();
  STAMP_AT(1);
  step(1, false, true, 0, 0);
  step(0, true, true, 0, 1);
  step(1, true, true, 0, 2);
  step(0, true, true, 0, 3);
  BAR();
  STAMP_AT(2);
  for (int s = 1; s < 15; ++s) {
    const int so = (s & 3) * G1_SLOT;
    step(1, true, true, so, 0);
    step(0, true, true, so, 1);
    step(1, true, true, so, 2);
    step(0, true, true, so, 3);
    BAR();
  }
  STAMP_AT(5);
  { const int so = 3 * G1_SLOT;
    step(1, true, true, so, 0);
    step(0, true, true, so, 1);
    step(1, true, true, so, 2);
    step(0, true, true, so, 3);
    step(1, true, false, so, 0); }
  STAMP_AT(3);
  const int head = hp * 2 + wn;
#pragma unroll
  for (int mt = 0; mt < 2; ++mt) {
    const int tile = tb * 4 + wm * 2 + mt;
    u16* qd = Qf + (size_t)((head * 64 + tile) * 2) * 512 + lane * 8;
    u16* kd = KVf + (size_t)((head * 64 + tile) * 4) * 512 + lane * 8;
    f32x16 qs;
#pragma unroll
    for (int r = 0; r < 16; ++r) qs[r] = acc[mt][0][r] * QSCALE;
    st16(qd, pack8(qs, 0)); st16(qd + 512, pack8(qs, 8));
    const bf16x8 k0 = pack8(acc[mt][1], 0), k1 = pack8(acc[mt][1], 8);
    st16(kd, k0); st16(kd + 512, k1);
    st16(kd + 1024, pack8(acc[mt][2], 0)); st16(kd + 1536, pack8(acc[mt][2], 8));
    float n2 = 0.f;
    const u32x4 a0 = __builtin_bit_cast(u32x4, k0), a1 = __builtin_bit_cast(u32x4, k1);
#pragma unroll
    for (int i = 0; i < 4; ++i) { float a = bflo(a0[i]), bb = bfhi(a0[i]), c = bflo(a1[i]), d = bfhi(a1[i]); n2 += a * a + bb * bb + c * c + d * d; }
    n2 += __shfl_xor(n2, 32);
#pragma unroll
    for (int o = 16; o >= 1; o >>= 1) n2 = fmaxf(n2, __shfl_xor(n2, o));
    if (lane == 0) kn2[head * 64 + tile] = n2;
  }
}

constexpr int A_SLOT = 32768, A_NSLOT = 3, A_LDS = A_SLOT * A_NSLOT;
template <int KS>
__global__ __launch_bounds__(512 * KS, 2 * KS) void k2_attn(const u16* __restrict__ Qf, const u16* __restrict__ KVf, const float* __restrict__ kn2, u16* __restrict__ Of) {
  extern __shared__ __attribute__((aligned(16))) char smem[];
  LAS char* lds = (LAS char*)smem;
  const int tid = threadIdx.x, lane = tid & 63, wid = __builtin_amdgcn_readfirstlane(tid >> 6);
  const int b = blockIdx.x, head = (b & 7) * 4 + (b >> 6), qb = (b >> 3) & 7;
  const int qt = wid & 7, kg = wid >> 3, hi = lane >> 5;
  constexpr int NPW = 4 / KS;
  constexpr int TPS = 8 / KS;
  const char* kvsrc = (const char*)KVf + (size_t)head * 64 * 4096 + wid * (NPW * 1024) + lane * 16;
  auto issue = [&](int st) {
    LAS char* dst = lds + (st % 3) * A_SLOT + wid * (NPW * 1024);
#pragma unroll
    for (int i = 0; i < NPW; ++i) glds16(kvsrc + (size_t)st * A_SLOT + i * 1024, dst + i * 1024);
  };
  issue(0); issue(1);
  const int qtile = qb * 8 + qt;
  const u16* qp = Qf + (size_t)((head * 64 + qtile) * 2) * 512 + lane * 8;
  const bf16x8 q0 = *(const bf16x8*)qp, q1 = *(const bf16x8*)(qp + 512);
  float kmax2 = kn2[head * 64 + lane];
#pragma unroll
  for (int o = 32; o >= 1; o >>= 1) kmax2 = fmaxf(kmax2, __shfl_xor(kmax2, o));
  float qn2 = 0.f;
  { const u32x4 a0 = __builtin_bit_cast(u32x4, q0), a1 = __builtin_bit_cast(u32x4, q1);
#pragma unroll
    for (int i = 0; i < 4; ++i) { float a = bflo(a0[i]), bb = bfhi(a0[i]), c = bflo(a1[i]), d = bfhi(a1[i]); qn2 += a * a + bb * bb + c * c + d * d; }
    qn2 += __shfl_xor(qn2, 32); }
  const float mref = __builtin_sqrtf(qn2 * kmax2) * 1.001f;
  f32x16 negm;
#pragma unroll
  for (int r = 0; r < 16; ++r) negm[r] = -mref;
  asm volatile("" : "+v"(negm));
  bf16x8 onesA;
  { const int row = lane & 15, kgp = lane >> 4; const short one = (short)0x3F80; const short v = ((row == 0 && !(kgp & 1)) || (row == 1 && (kgp & 1))) ? one : (short)0;
#pragma unroll
    for (int j = 0; j < 8; ++j) onesA[j] = v; }
  f32x16 O = f32x16{}; f32x4 L = f32x4{};
  f32x16 SA, SB;
  const int lb = lane * 16 + kg * (TPS * 4096);
  auto qk = [&](f32x16& Sd, int off) {
    lds_cptr p = lds + off + lb;
    const bf16x8 k0 = ldsb128(p), k1 = ldsb128(p + 1024);
    Sd = MFMA32(k0, q0, negm); Sd = MFMA32(k1, q1, Sd);
  };
  auto pv = [&](f32x16& Sc, int off) {
    lds_cptr p = lds + off + lb;
    const bf16x8 v0 = ldsb128(p + 2048), v1 = ldsb128(p + 3072);
#pragma unroll
    for (int r = 0; r < 16; ++r) Sc[r] = __builtin_amdgcn_exp2f(Sc[r]);
    const bf16x8 p0 = pack8(Sc, 0), p1 = pack8(Sc, 8);
    O = MFMA32(v0, p0, O); L = MFMA16(onesA, p0, L);
    O = MFMA32(v1, p1, O); L = MFMA16(onesA, p1, L);
  };
  WAITV(0); BAR();
  qk(SA, 0);
  for (int s = 0; s < 8; ++s) {
    if (s + 2 < 8) issue(s + 2);
    const int so = (s % 3) * A_SLOT, sn = ((s + 1) % 3) * A_SLOT;
#pragma unroll
    for (int i = 0; i < TPS; i += 2) {
      qk(SB, so + (i + 1) * 4096); pv(SA, so + i * 4096);
      if (i + 2 < TPS) qk(SA, so + (i + 2) * 4096); else if (s < 7) qk(SA, sn);
      pv(SB, so + (i + 1) * 4096);
    }
    if (s < 7) { WAITV(0); BAR(); }
  }
  const int r32 = lane & 31;
  float l0 = __shfl(L[0], r32 & 15), l1 = __shfl(L[1], r32 & 15);
  float l = r32 < 16 ? l0 : l1;
  if constexpr (KS == 2) {
    BAR();
    LAS float* red = (LAS float*)(lds) + qt * (17 * 64);
    if (kg == 1) {
#pragma unroll
      for (int r = 0; r < 16; ++r) red[r * 64 + lane] = O[r];
      red[16 * 64 + lane] = l;
    }
    BAR();
    if (kg == 1) return;
#pragma unroll
    for (int r = 0; r < 16; ++r) O[r] += red[r * 64 + lane];
    l += red[16 * 64 + lane];
  }
  if (__any(!(l >= 8.6736174e-19f && l < 3.0e38f))) {
    O = f32x16{}; float m = -3.0e38f; l = 0.f;
    const char* kvb = (const char*)KVf + (size_t)head * 64 * 4096 + lane * 16;
    for (int t = 0; t < 64; ++t) {
      const bf16x8 k0 = *(const bf16x8*)(kvb + t * 4096), k1 = *(const bf16x8*)(kvb + t * 4096 + 1024), v0 = *(const bf16x8*)(kvb + t * 4096 + 2048), v1 = *(const bf16x8*)(kvb + t * 4096 + 3072);
      f32x16 Sx = MFMA32(k0, q0, f32x16{}); Sx = MFMA32(k1, q1, Sx);
      float mx = Sx[0];
#pragma unroll
      for (int r = 1; r < 16; ++r) mx = fmaxf(mx, Sx[r]);
      mx = fmaxf(mx, __shfl_xor(mx, 32));
      const float mn = fmaxf(m, mx), al = __builtin_amdgcn_exp2f(m - mn);
      float rs = 0.f;
#pragma unroll
      for (int r = 0; r < 16; ++r) { Sx[r] = __builtin_amdgcn_exp2f(Sx[r] - mn); }
      const bf16x8 p0 = pack8(Sx, 0), p1 = pack8(Sx, 8);
      { const u32x4 a0 = __builtin_bit_cast(u32x4, p0), a1 = __builtin_bit_cast(u32x4, p1);
#pragma unroll
        for (int i = 0; i < 4; ++i) rs += bflo(a0[i]) + bfhi(a0[i]) + bflo(a1[i]) + bfhi(a1[i]); }
      rs += __shfl_xor(rs, 32);
      l = l * al + rs; m = mn;
#pragma unroll
      for (int r = 0; r < 16; ++r) O[r] *= al;
      O = MFMA32(v0, p0, O); O = MFMA32(v1, p1, O);
    }
  }
  const float rl = 1.0f / l;
#pragma unroll
  for (int r = 0; r < 16; ++r) O[r] *= rl;
  u16* od = Of + (size_t)((qtile * 32 + head) * 2) * 512 + lane * 8;
  *(bf16x8*)(od) = pack8(O, 0); *(bf16x8*)(od + 512) = pack8(O, 8);
}

constexpr int P_SLOT = 24576, P_NSLOT = 4, P_LDS = P_SLOT * P_NSLOT;
__global__ __launch_bounds__(512, 2) void k3_proj(const u16* __restrict__ Of, const u16* __restrict__ wo, u16* __restrict__ out) {
  extern __shared__ __attribute__((aligned(16))) char smem[];
  LAS char* lds = (LAS char*)smem;
  const int tid = threadIdx.x, lane = tid & 63, wid = __builtin_amdgcn_readfirstlane(tid >> 6);
  const int b = blockIdx.x, xcd = b & 7, idx = b >> 3;
  const int tb = xcd * 2 + (idx & 1), nb = idx >> 1;
  if (wid >= 4) {
    const int lw = wid - 4;
    const char* asrc = (const char*)Of + (size_t)(tb * 4 + lw) * 65536 + lane * 16;
    unsigned boff[2];
#pragma unroll
    for (int i = 0; i < 2; ++i) { const int row = 8 * (2 * lw + i) + (lane >> 3), ch = (lane & 7) ^ (((row >> 1) & 1) << 2); boff[i] = (unsigned)(row * 2048 + nb * 128 + ch * 16); }
    auto issue = [&](int st) {
      LAS char* sl = lds + (st & 3) * P_SLOT;
#pragma unroll
      for (int i = 0; i < 4; ++i) glds16(asrc + st * 4096 + i * 1024, sl + lw * 4096 + i * 1024);
#pragma unroll
      for (int i = 0; i < 2; ++i) glds16((const char*)wo + (size_t)st * 64 * 2048 + boff[i], sl + 16384 + (2 * lw + i) * 1024);
    };
    issue(0); issue(1); issue(2);
    WAITV(6); BAR();
    for (int s = 0; s < 15; ++s) {
      if (s + 3 < 16) { issue(s + 3); WAITV(6); } else { WAITV(0); }
      BAR();
    }
    BAR(); BAR();
  } else {
    const int wm = wid & 1, wn = wid >> 1, hi = lane >> 5;
    f32x16 acc[2]; acc[0] = f32x16{}; acc[1] = f32x16{};
    const int ab0 = wm * 8192 + lane * 16;
    const int q4 = (lane & 15) >> 2, p4 = lane & 3, g1 = (lane >> 4) & 1;
    const int wb0 = 16384 + (4 * hi + q4) * 128 + ((wn ^ (q4 >> 1)) << 6) + 32 * g1 + 8 * p4;
    bf16x8 af[2][2], wf[2];
    auto frags = [&](int buf, int so, int ks) {
      lds_cptr ap = lds + so + ab0 + ks * 1024;
      af[buf][0] = ldsb128(ap); af[buf][1] = ldsb128(ap + 4096);
      lds_cptr wp = lds + so + wb0 + ks * 2048;
      wf[buf] = cat8(ldstr(wp), ldstr(wp + 1024));
    };
    auto mma = [&](int buf) { acc[0] = MFMA32(wf[buf], af[buf][0], acc[0]); acc[1] = MFMA32(wf[buf], af[buf][1], acc[1]); };
    BAR();
    frags(0, 0, 0);
    for (int s = 0; s < 16; ++s) {
      const int so = (s & 3) * P_SLOT, sn = ((s + 1) & 3) * P_SLOT;
      frags(1, so, 1); mma(0);
      frags(0, so, 2); mma(1);
      frags(1, so, 3); mma(0);
      if (s < 15) frags(0, sn, 0);
      mma(1);
      if (s < 15) BAR();
    }
    BAR();
#pragma unroll
    for (int mt = 0; mt < 2; ++mt) {
      const int trow = wm * 64 + mt * 32 + (lane & 31);
#pragma unroll
      for (int g = 0; g < 4; ++g) {
        u32x2 pk = {cvtpk(acc[mt][4 * g], acc[mt][4 * g + 1]), cvtpk(acc[mt][4 * g + 2], acc[mt][4 * g + 3])};
        *(LAS u32x2*)(lds + trow * 144 + (wn * 32 + 8 * g + 4 * hi) * 2) = pk;
      }
    }
    BAR();
  }
#pragma unroll
  for (int i = 0; i < 2; ++i) {
    const int row = i * 64 + (tid >> 3), ch = tid & 7;
    const u32x4 v = *(const LAS u32x4*)(lds + row * 144 + ch * 16);
    *(u32x4*)(out + (size_t)(tb * 128 + row) * 2048 + 1024 + nb * 64 + ch * 8) = v;
  }
}

constexpr int C_CH = 20480, C_LDS = 4 * C_CH;
__global__ __launch_bounds__(256, 2) void k3_conv(const u16* __restrict__ kvT, const u16* __restrict__ hk, const u16* __restrict__ qin, const u16* __restrict__ Dp, u16* __restrict__ out) {
  extern __shared__ __attribute__((aligned(16))) char smem[];
  LAS char* lds = (LAS char*)smem;
  const int tid = threadIdx.x, lane = tid & 63, wid = __builtin_amdgcn_readfirstlane(tid >> 6);
  const int b = blockIdx.x, c0 = ((b & 7) * 32 + (b >> 3)) * 4, c = c0 + wid;
  LAS char* G = lds + wid * C_CH; LAS char* P = G + 16384;
#pragma unroll
  for (int i = 0; i < 4; ++i) glds16((const char*)kvT + (size_t)c * 4096 + i * 1024 + lane * 16, P + i * 1024);
#pragma unroll
  for (int i = 0; i < 4; ++i) {
    const int e = i * 64 + lane;
    const u16* src = hk + (size_t)c * S + 8 * e;
    const u32x4 w = *(const u32x4*)src;
    u32x2 pm = {0u, 0u}; if (e > 0) pm = *(const u32x2*)(src - 4);
    const unsigned wm2 = pm[0], wm1 = pm[1];
    const unsigned em1 = __builtin_amdgcn_alignbit(wm1, wm2, 16), e0 = __builtin_amdgcn_alignbit(w[0], wm1, 16), e1 = __builtin_amdgcn_alignbit(w[1], w[0], 16),
                   e2 = __builtin_amdgcn_alignbit(w[2], w[1], 16), e3 = __builtin_amdgcn_alignbit(w[3], w[2], 16);
    LAS char* d = G + (e >> 2) * 256 + (e & 3) * 16;
    *(LAS u32x4*)(d) = w;
    *(LAS u32x4*)(d + 64) = (u32x4){e0, e1, e2, e3};
    *(LAS u32x4*)(d + 128) = (u32x4){wm1, w[0], w[1], w[2]};
    *(LAS u32x4*)(d + 192) = (u32x4){em1, e0, e1, e2};
  }
  WAITV(0); asm volatile("s_waitcnt lgkmcnt(0)" ::: "memory");
  const int kq = lane >> 4, q4 = (lane & 15) >> 2, p4 = lane & 3, n16 = lane & 15;
  int abase[2];
#pragma unroll
  for (int u = 0; u < 2; ++u) { const int dl = 16 - 8 * kq - 4 * u + 4 * p4; abase[u] = (dl < 0 ? -256 + (dl + 32) * 2 : dl * 2) + q4 * 64; }
  const int bbase = -32 + 32 * n16 + 16 * kq;
  f32x4 acc[8];
#pragma unroll
  for (int t = 0; t < 8; ++t) acc[t] = f32x4{};
  for (int rho = 0; rho < 8; ++rho) {
    bf16x8 Bf[8];
#pragma unroll
    for (int nu = 1; nu < 8; ++nu) Bf[nu] = ldsb128(P + bbase - 64 * rho + 512 * nu);
    { const int off = bbase - 64 * rho; bf16x8 t = ldsb128(P + (off < 0 ? 0 : off)); Bf[0] = off < 0 ? bf16x8{} : t; }
#pragma unroll
    for (int mu = 0; mu < 8; ++mu) {
      bf16x8 Af;
      if (mu == 0) {
        const int o0 = abase[0] + 256 * rho, o1 = abase[1] + 256 * rho;
        s16x4 lo = ldstr(G + (o0 < 0 ? 0 : o0)), hi4 = ldstr(G + (o1 < 0 ? 0 : o1));
        const int d0 = 16 - 8 * kq + 4 * (n16 >> 2) + 32 * rho;
        if (d0 < 0) lo = s16x4{};
        if (d0 - 4 < 0) hi4 = s16x4{};
        Af = cat8(lo, hi4);
      } else {
        Af = cat8(ldstr(G + abase[0] + 256 * rho + 2048 * mu), ldstr(G + abase[1] + 256 * rho + 2048 * mu));
      }
#pragma unroll
      for (int nu = 0; nu + mu < 8; ++nu) acc[mu + nu] = MFMA16(Af, Bf[nu], acc[mu + nu]);
    }
  }
  const float dch = bflo((unsigned)Dp[c]);
  u32x2 wv[8];
#pragma unroll
  for (int T = 0; T < 8; ++T) {
    const int t = 256 * T + 16 * n16 + 4 * kq;
    const u32x2 kv = *(const LAS u32x2*)(P + t * 2);
    const float z0 = rbf(bflo(kv[0]) * dch), z1 = rbf(bfhi(kv[0]) * dch), z2 = rbf(bflo(kv[1]) * dch), z3 = rbf(bfhi(kv[1]) * dch);
    wv[T][0] = cvtpk(rbf(acc[T][0]) + z0, rbf(acc[T][1]) + z1); wv[T][1] = cvtpk(rbf(acc[T][2]) + z2, rbf(acc[T][3]) + z3);
  }
  __syncthreads();
#pragma unroll
  for (int T = 0; T < 8; ++T) { const int t = 256 * T + 16 * n16 + 4 * kq; *(LAS u32x2*)(lds + wid * 4160 + t * 2) = wv[T]; }
  __syncthreads();
#pragma unroll
  for (int i = 0; i < 8; ++i) {
    const int tblk = (i * 4 + wid) * 64;
    const s16x4 w4 = ldstr(lds + q4 * 4160 + (tblk + 16 * kq + 4 * p4) * 2);
    const int t = tblk + lane;
    const u32x2 qv = *(const u32x2*)(qin + (size_t)t * D + c0);
    const u32x2 wu = __builtin_bit_cast(u32x2, w4);
    u32x2 o = {cvtpk(bflo(qv[0]) * bflo(wu[0]), bfhi(qv[0]) * bfhi(wu[0])), cvtpk(bflo(qv[1]) * bflo(wu[1]), bfhi(qv[1]) * bfhi(wu[1]))};
    *(u32x2*)(out + (size_t)t * 2048 + c0) = o;
  }
}

#define KM(i) true
#define ATT_KS 1
extern "C" void kernel_launch(void* const* d_in, const int* in_sizes, int n_in, void* d_out, int out_size, void* d_ws, size_t ws_size, hipStream_t stream) {
  const u16* q = (const u16*)d_in[0]; const u16* k = (const u16*)d_in[1]; const u16* v = (const u16*)d_in[2];
  const u16* sk = (const u16*)d_in[3]; const u16* hk = (const u16*)d_in[4]; const u16* Dp = (const u16*)d_in[5];
  const u16* x = (const u16*)d_in[6]; const u16* wqkv = (const u16*)d_in[7]; const u16* wo = (const u16*)d_in[8];
  u16* out = (u16*)d_out; char* ws = (char*)d_ws;
  u16* Qf = (u16*)ws; u16* KVf = (u16*)(ws + (4 << 20)); u16* Of = (u16*)(ws + (12 << 20)); u16* kvT = (u16*)(ws + (16 << 20)); float* kn2 = (float*)(ws + (20 << 20));
  hipFuncSetAttribute((const void*)k1_qkv, hipFuncAttributeMaxDynamicSharedMemorySize, G1_LDS);
  hipFuncSetAttribute((const void*)k2_attn<ATT_KS>, hipFuncAttributeMaxDynamicSharedMemorySize, A_LDS);
  hipFuncSetAttribute((const void*)k3_proj, hipFuncAttributeMaxDynamicSharedMemorySize, P_LDS);
  hipFuncSetAttribute((const void*)k3_conv, hipFuncAttributeMaxDynamicSharedMemorySize, C_LDS);
  if (KM(0)) k1_qkv<<<256, 640, G1_LDS, stream>>>(x, wqkv, k, v, sk, Qf, KVf, kn2, kvT);
  if (KM(1)) k2_attn<ATT_KS><<<256, 512 * ATT_KS, A_LDS, stream>>>(Qf, KVf, kn2, Of);
  if (KM(2)) k3_proj<<<256, 512, P_LDS, stream>>>(Of, wo, out);
  if (KM(3)) k3_conv<<<256, 256, C_LDS, stream>>>(kvT, hk, q, Dp, out);
}
```

```cpp
#include <hip/hip_runtime.h>
#include <hip/hip_bf16.h>
#include <stdint.h>
#include <type_traits>

typedef unsigned short u16;
using bf16x8 = __attribute__((ext_vector_type(8))) short;
using s16x4  = __attribute__((ext_vector_type(4))) short;
using f32x16 = __attribute__((ext_vector_type(16))) float;
using f32x4  = __attribute__((ext_vector_type(4))) float;
using u32x4  = __attribute__((ext_vector_type(4))) unsigned;
using u32x2  = __attribute__((ext_vector_type(2))) unsigned;
typedef short v4i16_t __attribute__((ext_vector_type(4)));
#define LAS __attribute__((address_space(3)))
#define GAS __attribute__((address_space(1)))
#define DI __device__ __forceinline__
typedef LAS const char* lds_cptr;

constexpr int S = 2048, D = 1024, H = 32;
constexpr float QSCALE = 0.17677669529663687f * 1.4426950408889634f;

#define MFMA32(a, b, c) __builtin_amdgcn_mfma_f32_32x32x16_bf16((a), (b), (c), 0, 0, 0)
#define MFMA16(a, b, c) __builtin_amdgcn_mfma_f32_16x16x32_bf16((a), (b), (c), 0, 0, 0)

typedef float f32x2_t __attribute__((ext_vector_type(2)));
typedef __bf16 bf16x2_t __attribute__((ext_vector_type(2)));
DI unsigned cvtpk(float lo, float hi) { f32x2_t v = {lo, hi}; bf16x2_t b = __builtin_convertvector(v, bf16x2_t); return __builtin_bit_cast(unsigned, b); }
DI float bflo(unsigned p) { return __uint_as_float(p << 16); }
DI float bfhi(unsigned p) { return __uint_as_float(p & 0xffff0000u); }
DI float rbf(float x) { return bflo(cvtpk(x, x)); }
DI bf16x8 ldsb128(lds_cptr p) { return *(const LAS bf16x8*)p; }
DI s16x4 ldstr(lds_cptr p) { return __builtin_bit_cast(s16x4, __builtin_amdgcn_ds_read_tr16_b64_v4i16((LAS v4i16_t*)p)); }
DI bf16x8 cat8(s16x4 lo, s16x4 hi) { return __builtin_shufflevector(lo, hi, 0, 1, 2, 3, 4, 5, 6, 7); }
DI void glds16(const void* g, LAS char* l) { __builtin_amdgcn_global_load_lds((const GAS unsigned*)g, (LAS unsigned*)l, 16, 0, 0); }
DI bf16x8 pack8(const f32x16& x, int b) {
  u32x4 p; p[0] = cvtpk(x[b], x[b + 1]); p[1] = cvtpk(x[b + 2], x[b + 3]); p[2] = cvtpk(x[b + 4], x[b + 5]); p[3] = cvtpk(x[b + 6], x[b + 7]);
  return __builtin_bit_cast(bf16x8, p);
}
DI void bdma(__amdgpu_buffer_rsrc_t srd, unsigned voff, unsigned soff, unsigned m0v) {
  asm volatile("s_mov_b32 m0, %0\n\ts_nop 0\n\tbuffer_load_dwordx4 %1, %2, %3 offen lds" :: "s"(m0v), "v"(voff), "s"(srd), "s"(soff) : "m0", "memory");
}
#define RSRC(p, bytes) __builtin_amdgcn_make_buffer_rsrc((void*)(p), (short)0, (int)(bytes), 0x00020000)
#define WAITV(n) asm volatile("s_waitcnt vmcnt(" #n ")" ::: "memory")
#define BAR() asm volatile("s_waitcnt lgkmcnt(0)\n\ts_barrier" ::: "memory")

#define STAMP_DECL
#define STAMP_AT(i)
#define STAMP_RT(i)
#define STAMP_OUT(buf, kern)
#define TS_BEGIN(tsb, kern)
#define TS_END()
#define WT_SC1 1
#define PRE_MAP 0
#define G_MAP 0
template <int MAP> DI void patch_of(int b, int w, int& tp, int& cp) {
  const int gw = b * 2 + w;
  if (MAP == 0) { tp = gw >> 4; cp = gw & 15; }
  else if (MAP == 1) { tp = gw >> 4; cp = (gw + tp) & 15; }
  else { const int j = (b >> 3) * 2 + w; tp = (b & 7) * 4 + (j >> 4); cp = j & 15; }
}
#define PF_CODE_WGS 2
#define PF_CODE 3
#define PF_SELF 0
#define PF_FULL 1
#define CODE_PF(dst, sym, line) do { unsigned long long pc_; int off_; \
    asm volatile("s_getpc_b64 %0\n\ts_mov_b32 %1, " sym "@rel32@lo+4" : "=s"(pc_), "=s"(off_)); \
    const char* pc__ = (const char*)pc_ + off_ + (line) * 128; \
    asm volatile("global_load_dword %0, %1, off" : "+v"(dst) : "v"(pc__) : "memory"); } while (0)
#define SB() __builtin_amdgcn_sched_barrier(0)
#define BARS() do { SB(); BAR(); SB(); } while (0)
#define ST_MODE 2
DI void st16(void* p, bf16x8 v) {
  asm volatile("global_store_dwordx4 %0, %1, off sc1\n\ts_nop 1" :: "v"(p), "v"(v) : "memory");
}
DI void st8(void* p, u32x2 v) {
  asm volatile("global_store_dwordx2 %0, %1, off sc1" :: "v"(p), "v"(v) : "memory");
}
#define INIT_ST 2
#define LD_UNROLL 1
#define RDPAT 1
#define PRIO 0
#define USE_BUF 1
#define PRE_AT 0
#define PF_MODE 1
#define W_PERM 3
#define HPS(hp, sec) ((((hp) & ~3) + 4 * (sec) + (((hp) + (sec)) & 3)) & 15)
#define PF_FROM 4
#define PF_SLEEP0 0
#define PF_SLEEP 0
#define ROTM 24
#define ROT_EXPR (4 * (xcd >> 1) + 2 * (xcd & 1))
#define PRE_LD 12
#define PRE_C0 14
#define PF_GAP 3
#define PF_DIST 6
#define PFETCH 0
#define PF_H 1
#define PFW 5
#define PFW_AT 1
#define PRE_PF 0
#define PRE_SPREAD 0
#define PRE_N 3
constexpr int G1_SLOT = 40960, G1_NSLOT = 4, G1_LDS = G1_SLOT * G1_NSLOT;
extern "C" __global__ __launch_bounds__(PFETCH ? 704 : 640, 3) void k1_qkv(const u16* __restrict__ x, const u16* __restrict__ w, const u16* __restrict__ kin, const u16* __restrict__ vin,
                                                 const u16* __restrict__ sk, u16* __restrict__ Qf, u16* __restrict__ KVf, float* __restrict__ kn2, u16* __restrict__ kvT,
                                                 const u16* __restrict__ hk) {
  extern __shared__ __attribute__((aligned(16))) char smem[];
  LAS char* lds = (LAS char*)smem;
  const int tid = threadIdx.x, lane = tid & 63, wid = __builtin_amdgcn_readfirstlane(tid >> 6);
  const int b = blockIdx.x, xcd = b & 7, idx = b >> 3;
  TS_BEGIN((char*)kn2 + (6 << 20), 0)
#define HP_ROT 0
#define HP_XOR 0
  unsigned pfs = 0;
  if (lane < 10) CODE_PF(pfs, "k1_qkv", wid * 10 + lane);
  const int tb = (xcd & 1) * 8 + (idx & 7), hp = (((xcd >> 1) * 4) ^ HP_XOR) + (((idx >> 3) + HP_ROT) & 3);
  if (wid >= 8) {
    int tp, cp; patch_of<PRE_MAP>(b, wid - 8, tp, cp);
    const int t0 = tp * 64 + 8 * (lane >> 3), c0 = cp * 64 + 8 * (lane & 7);
    u32x4 kr[11], vr[8], sw[4];
    const u16* kp = kin + (size_t)t0 * D + c0; const u16* vp = vin + (size_t)t0 * D + c0;
#define TLBW 0
#define PFKV 0
#define PGT2 0
#define TLB_DONE 0
    unsigned pw0 = 0, pw1 = 0, pw2 = 0;
    {
      const char* pg = (const char*)w + (size_t)(idx * 48 + (wid - 8) * 24 + lane) * 4096 + xcd * 512;
      if (lane < 24) asm volatile("global_load_dword %0, %1, off" : "+v"(pw2) : "v"(pg) : "memory"); }
#pragma unroll
    for (int i = TLB_DONE; i < PFW_AT; ++i) BARS();
    { const int g = b * 2 + (wid - 8);
      const char* p0 = (const char*)w + (size_t)(g * 96 + lane) * 128; const char* p1 = p0 + 64 * 128; const char* p2 = (const char*)x + (size_t)(g * 64 + lane) * 128;
      asm volatile("global_load_dword %0, %1, off" : "+v"(pw0) : "v"(p0) : "memory");
      if (lane < 32) asm volatile("global_load_dword %0, %1, off" : "+v"(pw1) : "v"(p1) : "memory");
      if (PFW == 2) asm volatile("global_load_dword %0, %1, off" : "+v"(pw2) : "v"(p2) : "memory"); }
#pragma unroll
    for (int i = PFW_AT; i < PRE_LD; ++i) BARS();
    asm volatile("s_waitcnt vmcnt(0)" : "+v"(pw0), "+v"(pw1), "+v"(pw2) :: "memory");
    asm volatile("" : "+v"(pfs));
    unsigned pfh = 0;
    { const char* ph = (const char*)hk + (size_t)((b & 7) * 32 + (b >> 3)) * 16384 + (wid - 8) * 8192 + lane * 128;
      asm volatile("global_load_dword %0, %1, off" : "+v"(pfh) : "v"(ph) : "memory"); }
    unsigned pfc = 0;
    if (lane < (PF_FULL ? 41 : 32)) CODE_PF(pfc, "k2_attn", (wid - 8) * (PF_FULL ? 41 : 32) + lane);
#pragma unroll
    for (int i = 0; i < 4; ++i) sw[i] = *(const u32x4*)(sk + (size_t)c0 * 4 + 8 * i);
#pragma unroll
    for (int i = 0; i < 11; ++i) kr[i] = *(const u32x4*)(t0 - 3 + i < 0 ? kp : kp + (i - 3) * D);
#pragma unroll
    for (int i = 0; i < 8; ++i) vr[i] = *(const u32x4*)(vp + i * D);
#pragma unroll
    for (int i = PRE_LD; i < PRE_C0; ++i) BARS();
#pragma unroll
    for (int i = 0; i < 11; ++i) asm volatile("" : "+v"(kr[i]));
#pragma unroll
    for (int i = 0; i < 8; ++i) asm volatile("" : "+v"(vr[i]));
#pragma unroll
    for (int i = 0; i < 4; ++i) asm volatile("" : "+v"(sw[i]));
#pragma unroll
    for (int i = 0; i < 3; ++i) if (t0 - 3 + i < 0) kr[i] = (u32x4){0, 0, 0, 0};
    asm volatile("s_waitcnt vmcnt(0)" : "+v"(pfh), "+v"(pfc) :: "memory");
#pragma unroll
    for (int cp2 = 0; cp2 < 4; ++cp2) {
      f32x2_t wt[4];
      wt[0] = (f32x2_t){bflo(sw[cp2][0]), bflo(sw[cp2][2])}; wt[1] = (f32x2_t){bfhi(sw[cp2][0]), bfhi(sw[cp2][2])};
      wt[2] = (f32x2_t){bflo(sw[cp2][1]), bflo(sw[cp2][3])}; wt[3] = (f32x2_t){bfhi(sw[cp2][1]), bfhi(sw[cp2][3])};
      f32x2_t o[8];
#pragma unroll
      for (int h2 = 0; h2 < 2; ++h2) {
        f32x2_t kk[7];
#pragma unroll
        for (int i = 0; i < 7; ++i) kk[i] = (f32x2_t){bflo(kr[4 * h2 + i][cp2]), bfhi(kr[4 * h2 + i][cp2])};
#pragma unroll
        for (int i = 0; i < 4; ++i) {
          f32x2_t a = kk[i] * wt[3]; a = kk[i + 1] * wt[2] + a; a = kk[i + 2] * wt[1] + a; a = kk[i + 3] * wt[0] + a;
          o[4 * h2 + i] = a * (f32x2_t){bflo(vr[4 * h2 + i][cp2]), bfhi(vr[4 * h2 + i][cp2])};
        }
        if (PRE_C0 + 2 * cp2 + h2 < 16) BARS();
      }
#pragma unroll
      for (int e = 0; e < 2; ++e) {
        const u32x4 pk = {cvtpk(o[0][e], o[1][e]), cvtpk(o[2][e], o[3][e]), cvtpk(o[4][e], o[5][e]), cvtpk(o[6][e], o[7][e])};
        st16(kvT + (size_t)(c0 + 2 * cp2 + e) * S + t0, __builtin_bit_cast(bf16x8, pk));
      }
    }
#pragma unroll
    for (int i = PRE_C0 + 8; i < 16; ++i) BARS();
    TS_END();
    return;
  }
  if (wid >= 4) {
    const int lw = wid - 4;
    unsigned xoff[4], woff[6];
#pragma unroll
    for (int i = 0; i < 4; ++i) { const int row = 32 * lw + 8 * i + (lane >> 3), ch = (lane & 7) ^ ((row >> 1) & 7); xoff[i] = (unsigned)((tb * 128 + row) * 2048 + ch * 16); }
#pragma unroll
    for (int i = 0; i < 6; ++i) { const int p = 6 * lw + i, sec = p >> 3, row = 8 * (p & 7) + (lane >> 3), ch = (lane & 7) ^ (((row >> 1) & 1) << 2);
#define W_SHIFT 0
#define W_STRIDE 6144
      int grow = row, ghp = hp, gsec = sec;
      if (W_PERM == 1) grow = (p & 7) + 8 * (lane >> 3);
      if (W_PERM == 2) grow = (row & 3) * 16 + (row >> 2);
      if (W_PERM == 3) ghp = HPS(hp, sec);
      if (W_PERM == 4) { grow = 8 * (p & 7) + (lane >> 3); gsec = (sec + (lane >> 3)) % 3; }
      woff[i] = (unsigned)(grow * W_STRIDE + gsec * 2048 + ghp * 128 + ch * 16 + W_SHIFT); }
    const __amdgpu_buffer_rsrc_t rx = RSRC(x, S * D * 2), rw = RSRC(w, D * 3 * D * 2);
    const int rot = ROT_EXPR;
    auto issue = [&](int st) {
      const unsigned sl = (unsigned)(st & 3) * G1_SLOT + 4096u * lw, sw_ = (unsigned)(st & 3) * G1_SLOT + 16384u + 6144u * lw;
      st = (st + rot) & 15;
#pragma unroll
      for (int i = 0; i < 4; ++i) bdma(rx, xoff[i], (unsigned)st * 128u, sl + i * 1024);
#pragma unroll
      for (int i = 0; i < 6; ++i) bdma(rw, woff[i], (unsigned)st * (64u * W_STRIDE), sw_ + i * 1024);
    };
    issue(0); issue(1); WAITV(10);
    asm volatile("" : "+v"(pfs));
    BAR(); issue(2); issue(3);
    WAITV(20); BAR();
#pragma unroll LD_UNROLL
    for (int s = 1; s < 13; ++s) { issue(s + 3); WAITV(20); BAR(); }
    WAITV(10); BAR();
    WAITV(0); BAR();
    TS_END();
    return;
  }
  STAMP_DECL STAMP_RT(6); STAMP_AT(0);
  const int wm = wid & 1, wn = wid >> 1, r32 = lane & 31, hi = lane >> 5;
  f32x16 acc[2][3];
#pragma unroll
  for (int i = 0; i < 2; ++i)
#pragma unroll
    for (int j = 0; j < 3; ++j) acc[i][j] = f32x16{};
  const int xrow = wm * 64 + r32, xsw = (xrow >> 1) & 7;
  const int xb0 = xrow * 128 + ((hi ^ xsw) << 4);
  const int q4 = (lane & 15) >> 2, p4 = lane & 3, g1 = (lane >> 4) & 1;
  const int wb0 = 16384 + (8 * hi + q4) * 128 + ((wn ^ (q4 >> 1)) << 6) + 32 * g1 + 8 * p4;
  bf16x8 xa[2][2]; s16x4 wl[2][3], wh[2][3];
#define RD(...) __VA_ARGS__
  auto step = [&](int cur, bool mm, bool rd, int so, int ks) {
    const int nx = cur ^ 1;
    lds_cptr xp = lds + so + (xb0 ^ (ks << 5));
    lds_cptr wp = lds + so + wb0 + ks * 2048;
    bf16x8 w0, w1, w2;
    if (mm) { w0 = cat8(wl[cur][0], wh[cur][0]); w1 = cat8(wl[cur][1], wh[cur][1]); w2 = cat8(wl[cur][2], wh[cur][2]); }
#define RX0 xa[nx][0] = ldsb128(xp);
#define RX1 xa[nx][1] = ldsb128(xp + 4096);
#define RWL(i) wl[nx][i] = ldstr(wp + (i) * 8192);
#define RWH(i) wh[nx][i] = ldstr(wp + (i) * 8192 + 512);
#define G0 RX0 RWL(0)
#define G1 RWH(0) RWL(1)
#define G2 RWH(1) RWL(2)
#define G3 RWH(2) RX1
#define G4
#define G5
    if (mm) acc[0][0] = MFMA32(w0, xa[cur][0], acc[0][0]);
    SB(); if (rd) { RD(G0) } SB();
    if (mm) acc[0][1] = MFMA32(w1, xa[cur][0], acc[0][1]);
    SB(); if (rd) { RD(G1) } SB();
    if (mm) acc[0][2] = MFMA32(xa[cur][0], w2, acc[0][2]);
    SB(); if (rd) { RD(G2) } SB();
    if (mm) acc[1][0] = MFMA32(w0, xa[cur][1], acc[1][0]);
    SB(); if (rd) { RD(G3) } SB();
    if (mm) acc[1][1] = MFMA32(w1, xa[cur][1], acc[1][1]);
    SB(); if (rd) { RD(G4) } SB();
    if (mm) acc[1][2] = MFMA32(xa[cur][1], w2, acc[1][2]);
    SB(); if (rd) { RD(G5) } SB();
  };
  BAR();
  asm volatile("s_waitcnt vmcnt(0)" : "+v"(pfs) :: "memory");
  STAMP_AT(1);
  step(1, false, true, 0, 0);
  step(0, true, true, 0, 1);
  step(1, true, true, 0, 2);
  step(0, true, true, 0, 3);
  BAR();
  STAMP_AT(2);
  for (int s = 1; s < 15; ++s) {
    const int so = (s & 3) * G1_SLOT;
    step(1, true, true, so, 0);
    step(0, true, true, so, 1);
    step(1, true, true, so, 2);
    step(0, true, true, so, 3);
    BAR();
  }
  { const int so = 3 * G1_SLOT;
    step(1, true, true, so, 0);
    step(0, true, true, so, 1);
    step(1, true, true, so, 2);
    step(0, true, true, so, 3);
    step(1, true, false, so, 0); }
  STAMP_AT(3);
  const int head = hp * 2 + wn, headk = HPS(hp, 1) * 2 + wn, headv = HPS(hp, 2) * 2 + wn;
#pragma unroll
  for (int mt = 0; mt < 2; ++mt) {
    const int tile = tb * 4 + wm * 2 + mt;
    u16* qd = Qf + (size_t)((head * 64 + tile) * 2) * 512 + lane * 8;
    u16* kd = KVf + (size_t)((headk * 64 + tile) * 4) * 512 + lane * 8;
    u16* vd = KVf + (size_t)((headv * 64 + tile) * 4) * 512 + lane * 8;
    f32x16 qs;
#pragma unroll
    for (int r = 0; r < 16; ++r) qs[r] = acc[mt][0][r] * QSCALE;
    st16(qd, pack8(qs, 0)); st16(qd + 512, pack8(qs, 8));
    const bf16x8 k0 = pack8(acc[mt][1], 0), k1 = pack8(acc[mt][1], 8);
    st16(kd, k0); st16(kd + 512, k1);
    st16(vd + 1024, pack8(acc[mt][2], 0)); st16(vd + 1536, pack8(acc[mt][2], 8));
    float n2 = 0.f;
    const u32x4 a0 = __builtin_bit_cast(u32x4, k0), a1 = __builtin_bit_cast(u32x4, k1);
#pragma unroll
    for (int i = 0; i < 4; ++i) { float a = bflo(a0[i]), bb = bfhi(a0[i]), c = bflo(a1[i]), d = bfhi(a1[i]); n2 += a * a + bb * bb + c * c + d * d; }
    n2 += __shfl_xor(n2, 32);
#pragma unroll
    for (int o = 16; o >= 1; o >>= 1) n2 = fmaxf(n2, __shfl_xor(n2, o));
    if (lane == 0) kn2[headk * 64 + tile] = n2;
  }
  TS_END();
}

#define KS 0
#define TLBW2 0
constexpr int A_SLOT = 32768, A_RING = 2 * A_SLOT, C_GPAD = 256, C_PPAD = 512, C_CH = C_GPAD + 16384 + C_PPAD + 4096, A_XL = A_RING + 4 * C_CH,
              A_LDS = A_XL + (KS ? 8 * 512 + 64 : 0);
#define LP(x) ((LAS char*)(x))
#define APRIO 0
#define CPRIO 0
#define PF_K3 6
#define P_MAP 1
#define PF_WHAT 3
#define I1_MODE 3
#define APS_ON 1
#define APS 0x32211110
#define PREP_PRIO 0
#define C1_END 1
template <int N> DI void waitv() { asm volatile("s_waitcnt vmcnt(%0)" :: "n"(N) : "memory"); }
#define CPRIO0 3
#define CONV_UNR 1
#define A_UNR 8
#define CP_BOOST 9
#define CPRIO2 3
#define GSWZ 1
#define GSW(j, b) ((j) ^ (b))
#define CP_MODE 0
#define CPACE()
template <int CTRL, int RM> DI float dpp_max(float x) { const int y = __builtin_amdgcn_update_dpp(0, __float_as_int(x), CTRL, RM, 0xf, false); return fmaxf(x, __int_as_float(y)); }
extern "C" __global__ __launch_bounds__(768, 3) void k2_attn(const u16* __restrict__ Qf, const u16* __restrict__ KVf, const float* __restrict__ kn2, u16* __restrict__ Of,
                                                  const u16* __restrict__ kvT, const u16* __restrict__ hk, const u16* __restrict__ Dp, u16* __restrict__ wT,
                                                  const u16* __restrict__ wo, const u16* __restrict__ qin
                                                  ) {
  extern __shared__ __attribute__((aligned(16))) char smem[];
  LAS char* lds = (LAS char*)smem;
  const int tid = threadIdx.x, lane = tid & 63, wid = __builtin_amdgcn_readfirstlane(tid >> 6);
  const int b = blockIdx.x, head = (b & 7) * 4 + (b >> 6), qb = (b >> 3) & 7;
  TS_BEGIN((char*)kn2 + (6 << 20), 1)
  if (wid >= 8) {
    __builtin_amdgcn_s_setprio(CPRIO);
    const int lw = wid - 8;
    const int c = ((b & 7) * 32 + (b >> 3)) * 4 + lw;
    const __amdgpu_buffer_rsrc_t rkv = RSRC((const char*)KVf + (size_t)head * 64 * 4096, 64 * 4096);
    const int tadd = lw < 2 ? 2 : lw == 2 ? 0 : 1;
    const unsigned voff = lane * 16 + (lw == 0 ? 0 : lw == 1 ? 1024 : lw == 2 ? 3072 : 2048);
    auto issue = [&](int st) {
#pragma unroll
      for (int j = 0; j < 8; ++j) { const int t = KS ? (j >> 2) * 32 + st * 4 + (j & 3) + tadd : st * 8 + j + tadd; bdma(rkv, voff, (unsigned)(t > 63 ? 63 : t) * 4096u, (unsigned)((st & 1) * A_SLOT + j * 4096 + lw * 1024)); }
    };
#define S4()
#define S4L(it) BARS()
#define S7(i)
    S4();
    auto mini = [&]() {
      if (lw < 3) bdma(rkv, lane * 16 + lw * 1024, 0u, (unsigned)(A_SLOT + lw * 1024));
      if (lw < (KS ? 3 : 2)) bdma(rkv, lane * 16 + lw * 1024, KS ? 32 * 4096u : 4096u, (unsigned)(A_SLOT + 4096 + lw * 1024)); };
    const unsigned Gb = A_RING + lw * C_CH + C_GPAD, Pb = Gb + 16384 + C_PPAD;
    u32x4 hw[4];
    auto ldh = [&]() { const __amdgpu_buffer_rsrc_t rh = RSRC((const char*)hk + (size_t)c * 4096, 4096);
      const unsigned hv = lane * 16;
#pragma unroll
      for (int i = 0; i < 4; ++i) asm volatile("buffer_load_dwordx4 %0, %1, %2, 0 offen offset:%3" : "=v"(hw[i]) : "v"(hv), "s"(rh), "n"(i * 1024) : "memory"); };
    auto ldp = [&]() { const __amdgpu_buffer_rsrc_t rp = RSRC((const char*)kvT + (size_t)c * 4096, 4096);
#pragma unroll
      for (int i = 0; i < 4; ++i) bdma(rp, lane * 16 + i * 1024, 0u, Pb + i * 1024); };
    auto prep = [&]() {
#pragma unroll
      for (int i = 0; i < 4; ++i) asm volatile("" : "+v"(hw[i]));
#pragma unroll
      for (int i = 0; i < 4; ++i) {
        const int e = i * 64 + lane;
        const u32x4 w = hw[i];
        const int o2 = i ? __builtin_amdgcn_readlane((int)hw[i ? i - 1 : 0][2], 63) : 0, o1 = i ? __builtin_amdgcn_readlane((int)hw[i ? i - 1 : 0][3], 63) : 0;
        const unsigned wm2 = (unsigned)__builtin_amdgcn_update_dpp(o2, (int)w[2], 0x138, 0xf, 0xf, false), wm1 = (unsigned)__builtin_amdgcn_update_dpp(o1, (int)w[3], 0x138, 0xf, 0xf, false);
        const unsigned em1 = __builtin_amdgcn_alignbit(wm1, wm2, 16), e0 = __builtin_amdgcn_alignbit(w[0], wm1, 16), e1 = __builtin_amdgcn_alignbit(w[1], w[0], 16),
                       e2 = __builtin_amdgcn_alignbit(w[2], w[1], 16), e3 = __builtin_amdgcn_alignbit(w[3], w[2], 16);
        const int sw = (lane >> 2) & 3;
        LAS char* d = LP(Gb) + (e >> 2) * 256 + (e & 3) * 16;
        *(LAS u32x4*)(d + (GSW(0, sw) << 6)) = w;
        *(LAS u32x4*)(d + (GSW(1, sw) << 6)) = (u32x4){e0, e1, e2, e3};
        *(LAS u32x4*)(d + (GSW(2, sw) << 6)) = (u32x4){wm1, w[0], w[1], w[2]};
        *(LAS u32x4*)(d + (GSW(3, sw) << 6)) = (u32x4){em1, e0, e1, e2};
      } };
    mini(); ldh(); issue(0); ldp();
    *(LAS unsigned*)(LP(Gb - C_GPAD) + lane * 4) = 0u;
    *(LAS u32x2*)(LP(Pb - C_PPAD) + lane * 8) = (u32x2){0u, 0u};
    __builtin_amdgcn_s_setprio(CPRIO0);
    WAITV(12);
    prep();
    BARS();
    WAITV(4); S4(); BARS(); S4();
    issue(1);
    S7(0); S7(1); S7(2);
    const int kq = lane >> 4, q4 = (lane & 15) >> 2, p4 = lane & 3, n16 = lane & 15;
    unsigned aa[2], acn[2];
#pragma unroll
    for (int u = 0; u < 2; ++u) { const int dl = 16 - 8 * kq - 4 * u + 4 * p4; aa[u] = Gb + (dl < 0 ? -256 + (dl + 32) * 2 : dl * 2) + (GSWZ ? 0 : q4 * 64); acn[u] = dl < 0 ? 192 : 0; }
    unsigned vB = Pb - 32 + 32 * n16 + 16 * kq;
    f32x4 acc[8];
#pragma unroll
    for (int t = 0; t < 8; ++t) acc[t] = f32x4{};
    WAITV(8);
    S7(3);
    const unsigned dbits = Dp[c];
    unsigned pf0 = 0, pf1 = 0, pf2 = 0, pf3 = 0;
#pragma unroll CONV_UNR
    for (int it = 1; it <= (I1_MODE == 5 ? 9 : 8); ++it) {
      if (it == C1_END) __builtin_amdgcn_s_setprio(CPRIO);
      if (it >= 2 && it < 8) issue(it);
      if (it == CP_BOOST) __builtin_amdgcn_s_setprio(CPRIO2);
      if (I1_MODE != 5 || it >= 2) {
      unsigned x0 = aa[0], x1 = aa[1];
      if (GSWZ) { x0 += (q4 * 64) ^ acn[0]; x1 += (q4 * 64) ^ acn[1]; acn[0] = (acn[0] + 64) & 192; acn[1] = (acn[1] + 64) & 192; }
      asm volatile("" : "+v"(x0), "+v"(x1), "+v"(vB));
      bf16x8 Bf[8];
#pragma unroll
      for (int nu = 0; nu < 8; ++nu) Bf[nu] = ldsb128(LP(vB) + 512 * nu);
#pragma unroll
      for (int mu = 0; mu < 8; ++mu) {
        const bf16x8 Af = cat8(ldstr(LP(x0) + 2048 * mu), ldstr(LP(x1) + 2048 * mu));
#pragma unroll
        for (int nu = 0; nu + mu < 8; ++nu) { acc[mu + nu] = MFMA16(Af, Bf[nu], acc[mu + nu]); CPACE(); }
      }
      aa[0] += 256; aa[1] += 256; vB -= 64;
      }
      if (it == PF_K3) {
        const int wi = (b >> 3) * 4 + lw;
        const char* pw = (const char*)wo + (size_t)(wi * 8 + (lane >> 3)) * 2048 + (b & 1) * 1024 + (lane & 7) * 128;
        if (PF_WHAT & 1) asm volatile("global_load_dword %0, %1, off" : "+v"(pf0) : "v"(pw) : "memory");
        int tq, cq; patch_of<G_MAP>(b, lw >> 1, tq, cq);
        const char* pq = (const char*)qin + (size_t)(tq * 64 + (lw & 1) * 32 + (lane & 31)) * 2048 + cq * 128;
        if (PF_WHAT & 2) { if (lane < 32) asm volatile("global_load_dword %0, %1, off" : "+v"(pf1) : "v"(pq) : "memory"); }
        if (lane < (PF_FULL ? 22 : 16)) CODE_PF(pf2, "k3_proj", lw * (PF_FULL ? 22 : 16) + lane);
      }
      if (it < 8) {
        if (PF_K3 && it == PF_K3) waitv<(PF_WHAT & 1) + ((PF_WHAT >> 1) & 1) + (PF_CODE ? 1 : 0)>(); else if (TLBW2 && it == TLBW2) waitv<1>(); else WAITV(0);
        if (TLBW2 && it == 7) asm volatile("s_waitcnt vmcnt(0)" ::: "memory");
        asm volatile("" : "+v"(pf0), "+v"(pf1), "+v"(pf2), "+v"(pf3)); S4L(it);
      }
    }
    { const float dch = bflo(dbits);
      u16* wd = wT + (size_t)c * S + 16 * n16 + 4 * kq;
#pragma unroll
      for (int T = 0; T < 8; ++T) {
        const u32x2 kv = *(const LAS u32x2*)(LP(Pb) + (256 * T + 16 * n16 + 4 * kq) * 2);
        const float z0 = rbf(bflo(kv[0]) * dch), z1 = rbf(bfhi(kv[0]) * dch), z2 = rbf(bflo(kv[1]) * dch), z3 = rbf(bfhi(kv[1]) * dch);
        u32x2 wv;
        wv[0] = cvtpk(rbf(acc[T][0]) + z0, rbf(acc[T][1]) + z1); wv[1] = cvtpk(rbf(acc[T][2]) + z2, rbf(acc[T][3]) + z3);
        st8(wd + 256 * T, wv);
      } }
    TS_END();
    return;
  }
  const int hi = lane >> 5;
#define S5()
  S5();
  const int qtile = qb * 8 + wid;
  const u16* qp = Qf + (size_t)((head * 64 + qtile) * 2) * 512 + lane * 8;
  const u16* kvb = KVf + (size_t)head * 64 * 2048 + lane * 8;
  const bf16x8 q0 = *(const bf16x8*)qp, q1 = *(const bf16x8*)(qp + 512);
  float kmax2 = kn2[head * 64 + lane];
  kmax2 = dpp_max<0x111, 0xf>(kmax2); kmax2 = dpp_max<0x112, 0xf>(kmax2); kmax2 = dpp_max<0x114, 0xf>(kmax2); kmax2 = dpp_max<0x118, 0xf>(kmax2);
  kmax2 = dpp_max<0x142, 0xa>(kmax2); kmax2 = dpp_max<0x143, 0xc>(kmax2);
  kmax2 = __int_as_float(__builtin_amdgcn_readlane(__float_as_int(kmax2), 63));
  float qn2 = 0.f;
  { const u32x4 a0 = __builtin_bit_cast(u32x4, q0), a1 = __builtin_bit_cast(u32x4, q1);
#pragma unroll
    for (int i = 0; i < 4; ++i) { float a = bflo(a0[i]), bb = bfhi(a0[i]), c = bflo(a1[i]), d = bfhi(a1[i]); qn2 += a * a + bb * bb + c * c + d * d; }
    const auto sw2 = __builtin_amdgcn_permlane32_swap(__float_as_uint(qn2), __float_as_uint(qn2), false, false);
    qn2 = __uint_as_float(sw2[0]) + __uint_as_float(sw2[1]); }
  const float mref = __builtin_sqrtf(qn2 * kmax2) * 1.001f;
  f32x16 negm;
#pragma unroll
  for (int r = 0; r < 16; ++r) negm[r] = -mref;
  asm volatile("" : "+v"(negm));
  bf16x8 onesA;
  { const int row = lane & 15, kgp = lane >> 4; const short one = (short)0x3F80; const short v = ((row == 0 && !(kgp & 1)) || (row == 1 && (kgp & 1))) ? one : (short)0;
#pragma unroll
    for (int j = 0; j < 8; ++j) onesA[j] = v; }
  f32x16 O = f32x16{}; f32x4 L = f32x4{};
  f32x16 SA, SB_;
  bf16x8 p0, p1 = bf16x8{};
  S5();
  BARS();
  const bf16x8 ka0 = ldsb128(lds + A_SLOT + lane * 16), ka1 = ldsb128(lds + A_SLOT + 1024 + lane * 16);
  bf16x8 K0 = ldsb128(lds + A_SLOT + 4096 + lane * 16), K1 = ldsb128(lds + A_SLOT + 4096 + 1024 + lane * 16), V0 = ldsb128(lds + A_SLOT + 2048 + lane * 16), V1 = bf16x8{};
  SA = MFMA32(ka0, q0, negm); SA = MFMA32(ka1, q1, SA);
#define EXP(Sx, r) Sx[r] = __builtin_amdgcn_exp2f(Sx[r])
#define ALD(dst, p) dst = ldsb128(p)
#define A_LMODE 3
  f32x2_t Lp0 = {0.f, 0.f}, Lp1 = {0.f, 0.f};
#define LMM(p)
#define LADD(r)
  typedef __bf16 bf16x8v __attribute__((ext_vector_type(8)));
  typedef __bf16 bf16x16v __attribute__((ext_vector_type(16)));
  bf16x8 onesS; int sidx;
  { const int row = lane & 15;
#pragma unroll
    for (int j = 0; j < 8; ++j) onesS[j] = (row < 4 && (((j >> 2) ^ row) & 1) == 0) ? (short)0x3F80 : (short)0;
    sidx = (row & 2) ? (int)0xEEEEEEEE : 0x44444444; }
#define LSP() L = __builtin_amdgcn_smfmac_f32_16x16x64_bf16(__builtin_bit_cast(bf16x8v, onesS), \
    __builtin_bit_cast(bf16x16v, __builtin_shufflevector(p1, p0, 0, 1, 2, 3, 4, 5, 6, 7, 8, 9, 10, 11, 12, 13, 14, 15)), L, sidx, 0, 0)
  auto step = [&](f32x16& Sc, f32x16& Sn, lds_cptr sp, bool bar) {
    Sn = MFMA32(K0, q0, negm); SB();
    EXP(Sc, 0); EXP(Sc, 1); EXP(Sc, 2); EXP(Sc, 3); SB();
    if (bar) BARS();
    ALD(K0, sp); SB();
    O = MFMA32(V1, p1, O); SB();
    EXP(Sc, 4); EXP(Sc, 5); EXP(Sc, 6); EXP(Sc, 7); LADD(0); SB();
    ALD(V1, sp + 2048); SB();
    Sn = MFMA32(K1, q1, Sn); SB();
    p0 = pack8(Sc, 0); EXP(Sc, 8); EXP(Sc, 9); LADD(4); SB();
    ALD(K1, sp + 1024); SB();
    LMM(p1); LSP(); SB();
    EXP(Sc, 10); EXP(Sc, 11); EXP(Sc, 12); EXP(Sc, 13); SB();
    O = MFMA32(V0, p0, O); SB();
    EXP(Sc, 14); EXP(Sc, 15); LADD(8); SB();
    ALD(V0, sp + 3072); SB();
    LMM(p0); SB();
    p1 = pack8(Sc, 8); LADD(12); SB();
  };
#define APSN(k) ((APS >> (4 * (7 - (k)))) & 3)
#define APSET(k) do { if (APS_ON && ((k) == 0 || APSN(k) != APSN((k) ? (k) - 1 : 0))) { __builtin_amdgcn_s_setprio(APSN(k)); SB(); } } while (0)
  for (int s = 0; s < 8; ++s) {
    lds_cptr sp = lds + (s & 1) * A_SLOT + lane * 16;
    APSET(0); step(SA, SB_, sp, true);
    APSET(1); step(SB_, SA, sp + 4096, false);
    APSET(2); step(SA, SB_, sp + 2 * 4096, false);
    APSET(3); step(SB_, SA, sp + 3 * 4096, false);
    APSET(4); step(SA, SB_, sp + 4 * 4096, false);
    APSET(5); step(SB_, SA, sp + 5 * 4096, false);
    APSET(6); step(SA, SB_, sp + 6 * 4096, false);
    APSET(7); step(SB_, SA, sp + 7 * 4096, false);
  }
  S5();
  O = MFMA32(V1, p1, O); p0 = bf16x8{}; LSP();
  const int r32 = lane & 31;
  float l0 = __shfl(L[0] + L[2], r32 & 15), l1 = __shfl(L[1] + L[3], r32 & 15);
  float l = r32 < 16 ? l0 : l1;
  if (__any(!(l >= 8.6736174e-19f && l < 3.0e38f))) {
    O = f32x16{}; float m = -3.0e38f; l = 0.f;
    for (int t = 0; t < 64; ++t) {
      const bf16x8 k0 = *(const bf16x8*)(kvb + t * 2048), k1 = *(const bf16x8*)(kvb + t * 2048 + 512), v0 = *(const bf16x8*)(kvb + t * 2048 + 1024), v1 = *(const bf16x8*)(kvb + t * 2048 + 1536);
      f32x16 Sx = MFMA32(k0, q0, f32x16{}); Sx = MFMA32(k1, q1, Sx);
      float mx = Sx[0];
#pragma unroll
      for (int r = 1; r < 16; ++r) mx = fmaxf(mx, Sx[r]);
      mx = fmaxf(mx, __shfl_xor(mx, 32));
      const float mn = fmaxf(m, mx), al = __builtin_amdgcn_exp2f(m - mn);
      float rs = 0.f;
#pragma unroll
      for (int r = 0; r < 16; ++r) { Sx[r] = __builtin_amdgcn_exp2f(Sx[r] - mn); }
      const bf16x8 x0 = pack8(Sx, 0), x1 = pack8(Sx, 8);
      { const u32x4 a0 = __builtin_bit_cast(u32x4, x0), a1 = __builtin_bit_cast(u32x4, x1);
#pragma unroll
        for (int i = 0; i < 4; ++i) rs += bflo(a0[i]) + bfhi(a0[i]) + bflo(a1[i]) + bfhi(a1[i]); }
      rs += __shfl_xor(rs, 32);
      l = l * al + rs; m = mn;
#pragma unroll
      for (int r = 0; r < 16; ++r) O[r] *= al;
      O = MFMA32(v0, x0, O); O = MFMA32(v1, x1, O);
    }
  }
  const float rl = 1.0f / l;
#pragma unroll
  for (int r = 0; r < 16; ++r) O[r] *= rl;
  u16* od = Of + (size_t)((qtile * 32 + head) * 2) * 512 + lane * 8;
  S5();
  st16(od, pack8(O, 0)); st16(od + 512, pack8(O, 8));
  TS_END();
}

#define GATE_LD 13
#define GATE_ST 16
#define P_NS 4
#define P_PRE 2
#define P_ROT 0
#define P_ORD 1
constexpr int P_STG = 131072, P_LDS = P_STG + 4 * 4608;
constexpr int p_issued(int s) { int a = P_PRE + 2 * (s + 1), c = s + P_NS - 1 + 1; if (s < 0) c = P_NS; int m = a < c ? a : c; return m < 16 ? m : 16; }
template <int S_> struct PIss { static constexpr int v = p_issued(S_); };
extern "C" __global__ __launch_bounds__(384, 2) void k3_proj(const u16* __restrict__ Of, const u16* __restrict__ wo, const u16* __restrict__ wT, const u16* __restrict__ qin, u16* __restrict__ out) {
  extern __shared__ __attribute__((aligned(16))) char smem[];
  LAS char* lds = (LAS char*)smem;
  const int tid = threadIdx.x, lane = tid & 63, wid = __builtin_amdgcn_readfirstlane(tid >> 6);
  const int b = blockIdx.x, xcd = b & 7, idx = b >> 3;
  const int tb = (xcd >> 1) * 4 + (idx & 3), nb = (xcd & 1) * 8 + (idx >> 2);
  const int rot = P_ROT ? (2 * (idx >> 2) + 4 * (idx & 3)) & 15 : 0;
  TS_BEGIN((char*)Of + (14 << 20), 2)
  if (wid >= 4) {
    int tp, cp; patch_of<G_MAP>(b, wid - 4, tp, cp);
    const int t0 = tp * 64 + 8 * (lane >> 3), c0 = cp * 64 + 8 * (lane & 7);
    u32x4 wr[8], qr[8];
#pragma unroll
    for (int i = 0; i < GATE_LD; ++i) BARS();
#pragma unroll
    for (int i = 0; i < 8; ++i) wr[i] = *(const u32x4*)(wT + (size_t)(c0 + i) * S + t0);
#pragma unroll
    for (int i = 0; i < 8; ++i) qr[i] = *(const u32x4*)(qin + (size_t)(t0 + i) * D + c0);
#pragma unroll
    for (int i = GATE_LD; i < GATE_ST; ++i) BARS();
#pragma unroll
    for (int i = 0; i < 8; ++i) asm volatile("" : "+v"(wr[i]), "+v"(qr[i]));
#pragma unroll
    for (int r = 0; r < 8; ++r) {
      u32x4 o;
#pragma unroll
      for (int j = 0; j < 4; ++j) {
        const unsigned a = wr[2 * j][r >> 1], bq = wr[2 * j + 1][r >> 1];
        const float w0 = (r & 1) ? bfhi(a) : bflo(a), w1 = (r & 1) ? bfhi(bq) : bflo(bq);
        o[j] = cvtpk(bflo(qr[r][j]) * w0, bfhi(qr[r][j]) * w1);
      }
      st16(out + (size_t)(t0 + r) * 2048 + c0, __builtin_bit_cast(bf16x8, o));
      if (GATE_ST + r < 15) BARS();
    }
    TS_END();
    return;
  }
#define S6()
  S6();
  const int hi = lane >> 5;
  const __amdgpu_buffer_rsrc_t ra = RSRC((const char*)Of + (size_t)(tb * 4 + wid) * 65536, 65536), rb = RSRC((const char*)wo + nb * 128, 1024 * 2048 - nb * 128);
  const unsigned avoff = lane * 16;
  unsigned bvoff[2];
#pragma unroll
  for (int i = 0; i < 2; ++i) { const int row = 8 * (2 * wid + i) + (lane >> 3), ch = (lane & 7) ^ (((row >> 1) & 1) << 2); bvoff[i] = (unsigned)(row * 2048 + ch * 16); }
  bf16x8 af[P_NS][4];
  auto issB = [&](int st, int i) { bdma(rb, bvoff[i], (unsigned)((st + rot) & 15) * (64u * 2048u), (unsigned)(st * 8192 + (2 * wid + i) * 1024)); };
  auto issA = [&](int st, int i) {
    asm volatile("buffer_load_dwordx4 %0, %1, %2, %3 offen offset:%4" : "=v"(af[st % P_NS][i]) : "v"(avoff), "s"(ra), "s"((unsigned)((st + rot) & 15) * 4096u), "n"(i * 1024) : "memory");
  };
  auto issue = [&](int st) { issB(st, 0); issA(st, 0); issB(st, 1); issA(st, 1); issA(st, 2); issA(st, 3); };
  f32x16 acc[2]; acc[0] = f32x16{}; acc[1] = f32x16{};
  const int q4 = (lane & 15) >> 2, p4 = lane & 3, g1 = (lane >> 4) & 1;
  const int wb0 = (4 * hi + q4) * 128 + ((q4 >> 1) << 6) + 32 * g1 + 8 * p4;
  s16x4 wl[2][4][2], wh[2][4][2];
  auto rdw = [&](int buf, int st, int ks) {
    lds_cptr wp0 = lds + st * 8192 + ks * 2048 + wb0, wp1 = lds + st * 8192 + ks * 2048 + (wb0 ^ 64);
    wl[buf][ks][0] = ldstr(wp0); wh[buf][ks][0] = ldstr(wp0 + 1024); wl[buf][ks][1] = ldstr(wp1); wh[buf][ks][1] = ldstr(wp1 + 1024);
  };
  auto mma1 = [&](int buf, int ks, int n, const bf16x8& a) { acc[n] = MFMA32(cat8(wl[buf][ks][n], wh[buf][ks][n]), a, acc[n]); };
  auto mma = [&](int buf, int ks, const bf16x8& a) {
    acc[0] = MFMA32(cat8(wl[buf][ks][0], wh[buf][ks][0]), a, acc[0]); acc[1] = MFMA32(cat8(wl[buf][ks][1], wh[buf][ks][1]), a, acc[1]);
  };
#pragma unroll
  for (int st = 0; st < p_issued(-1) && st < P_PRE; ++st) issue(st);
  S6();
  waitv<6 * (P_PRE - 1)>();
  S6(); BARS(); S6();
  auto iter = [&](auto sc) {
    constexpr int s = decltype(sc)::value;
    constexpr int cur = s % P_NS, prv = (s + P_NS - 1) % P_NS;
    constexpr int i0 = s == 0 ? P_PRE : p_issued(s - 1), i1 = p_issued(s);
    constexpr bool ld = i0 < i1;
#pragma unroll
    for (int j = 0; j < 8; ++j) {
      if (s > 0) mma1((s - 1) & 1, j >> 1, j & 1, af[prv][j >> 1]);
      SB();
      if (j < 4) { rdw(s & 1, s, j); SB(); }
      if (ld) {
        if (j == 0) issB(i0, 0);
        if (j == 1) issA(i0, 0);
        if (j == 2) issB(i0, 1);
        if (j == 3) issA(i0, 1);
        if (j == 5) issA(i0, 2);
        if (j == 7) issA(i0, 3);
        SB();
      }
    }
#pragma unroll
    for (int st = i0 + 1; st < i1; ++st) issue(st);
    SB();
    if (s < 15) {
      waitv<6 * (i1 - (s + 2) > 0 ? i1 - (s + 2) : 0)>();
#pragma unroll
      for (int i = 0; i < 4; ++i) asm volatile("" : "+v"(af[(s + 1) % P_NS][i]));
      BARS();
      S6();
    }
    (void)cur;
  };
#pragma unroll
  for (int i = 0; i < 4; ++i) asm volatile("" : "+v"(af[0][i]));
  iter(std::integral_constant<int, 0>{}); iter(std::integral_constant<int, 1>{}); iter(std::integral_constant<int, 2>{}); iter(std::integral_constant<int, 3>{});
  iter(std::integral_constant<int, 4>{}); iter(std::integral_constant<int, 5>{}); iter(std::integral_constant<int, 6>{}); iter(std::integral_constant<int, 7>{});
  iter(std::integral_constant<int, 8>{}); iter(std::integral_constant<int, 9>{}); iter(std::integral_constant<int, 10>{}); iter(std::integral_constant<int, 11>{});
  iter(std::integral_constant<int, 12>{}); iter(std::integral_constant<int, 13>{}); iter(std::integral_constant<int, 14>{}); iter(std::integral_constant<int, 15>{});
#pragma unroll
  for (int ks = 0; ks < 4; ++ks) mma(1, ks, af[15 % P_NS][ks]);
  S6();
  LAS char* sg = lds + P_STG + wid * 4608;
#pragma unroll
  for (int n = 0; n < 2; ++n)
#pragma unroll
    for (int g = 0; g < 4; ++g) {
      u32x2 pk = {cvtpk(acc[n][4 * g], acc[n][4 * g + 1]), cvtpk(acc[n][4 * g + 2], acc[n][4 * g + 3])};
      *(LAS u32x2*)(sg + (lane & 31) * 144 + (n * 32 + 8 * g + 4 * hi) * 2) = pk;
    }
  asm volatile("s_waitcnt lgkmcnt(0)" ::: "memory");
#pragma unroll
  for (int i = 0; i < 4; ++i) {
    const int row = i * 8 + (lane >> 3), ch = lane & 7;
    const bf16x8 v = *(const LAS bf16x8*)(sg + row * 144 + ch * 16);
    st16(out + (size_t)((tb * 4 + wid) * 32 + row) * 2048 + 1024 + nb * 64 + ch * 8, v);
  }
  TS_END();
}

#define KM(i) true
#define ATT_KS 1
extern "C" void kernel_launch(void* const* d_in, const int* in_sizes, int n_in, void* d_out, int out_size, void* d_ws, size_t ws_size, hipStream_t stream) {
  const u16* q = (const u16*)d_in[0]; const u16* k = (const u16*)d_in[1]; const u16* v = (const u16*)d_in[2];
  const u16* sk = (const u16*)d_in[3]; const u16* hk = (const u16*)d_in[4]; const u16* Dp = (const u16*)d_in[5];
  const u16* x = (const u16*)d_in[6]; const u16* wqkv = (const u16*)d_in[7]; const u16* wo = (const u16*)d_in[8];
  u16* out = (u16*)d_out; char* ws = (char*)d_ws;
  u16* Qf = (u16*)ws; u16* KVf = (u16*)(ws + (4 << 20)); u16* Of = (u16*)(ws + (12 << 20)); u16* kvT = (u16*)(ws + (16 << 20)); float* kn2 = (float*)(ws + (20 << 20)); u16* wT = (u16*)(ws + (28 << 20));
  hipFuncSetAttribute((const void*)k1_qkv, hipFuncAttributeMaxDynamicSharedMemorySize, G1_LDS);
  hipFuncSetAttribute((const void*)k2_attn, hipFuncAttributeMaxDynamicSharedMemorySize, A_LDS);
  hipFuncSetAttribute((const void*)k3_proj, hipFuncAttributeMaxDynamicSharedMemorySize, P_LDS);
  if (KM(0)) k1_qkv<<<256, PFETCH ? 704 : 640, G1_LDS, stream>>>(x, wqkv, k, v, sk, Qf, KVf, kn2, kvT, hk);
  if (KM(1)) k2_attn<<<256, 768, A_LDS, stream>>>(Qf, KVf, kn2, Of, kvT, hk, Dp, wT, wo, q
    );
  if (KM(2)) k3_proj<<<256, 384, P_LDS, stream>>>(Of, wo, wT, q, out);
}
```
